```python
import torch

def _softmax(x, axis=-1):
    return torch.softmax(x, dim=axis)
def _gelu(x):
    return torch.nn.functional.gelu(x, approximate="tanh")
def _silu(x):
    return torch.nn.functional.silu(x)
def _sigmoid(x):
    return torch.sigmoid(x)
def _layernorm(x, axis=-1, eps=1e-5):
    m = x.mean(dim=axis, keepdim=True)
    v = x.var(dim=axis, keepdim=True, unbiased=False)
    return (x - m) / torch.sqrt(v + eps)
def _scatter_add(tgt, idx, src, axis=0):
    return tgt.clone().index_add_(axis, idx, src)
def _segment_sum(src, seg, n):
    out = torch.zeros((n,) + src.shape[1:], dtype=src.dtype)
    return out.index_add_(0, seg, src)
def _pad(x, pw):
    flat = tuple(v for pair in reversed(pw) for v in pair)
    return torch.nn.functional.pad(x, flat)

kernel_name = 'recurrent_scan__mha_block_cached__swiglu_ffn__decode'

def setup_inputs():

    _w = (lambda a: (lambda t: t.to(torch.bfloat16) if t.is_floating_point() else t)(torch.as_tensor(a)))
    def _A_setup():
        B, Sq, D = 2, 1, 512
        H, Sk, Dh = 32, 16384, 128
        return {
            "x": _w((lambda *s: torch.randn(*s, dtype=torch.bfloat16) * 0.05)(B, Sq, D)),
            "k_cache": _w((lambda *s: torch.randn(*s, dtype=torch.bfloat16) * 1.6)(B, H, Sk, Dh)),
            "v_cache": _w((lambda *s: torch.randn(*s, dtype=torch.bfloat16) * 0.2)(B, H, Sk, Dh)),
            "wq": _w((lambda *s: torch.randn(*s, dtype=torch.bfloat16) * 1.6)(D, H * Dh) * (D ** -0.5)),
            "wo": _w((lambda *s: torch.randn(*s, dtype=torch.bfloat16) * 0.2)(H * Dh, D) * ((H * Dh) ** -0.5)),
        }
    _A_in = _A_setup()

    def _B_setup():
        B, S, D, F = 2, 1, 512, 1792
        return {
            "x": _w((lambda *s: torch.randn(*s, dtype=torch.bfloat16) * 0.05)(B, S, D)),
            "w_gate": _w((lambda *s: torch.randn(*s, dtype=torch.bfloat16) * 0.1)(D, F)),
            "w_up": _w((lambda *s: torch.randn(*s, dtype=torch.bfloat16) * 0.1)(D, F)),
            "w_down": _w((lambda *s: torch.randn(*s, dtype=torch.bfloat16) * 0.1)(F, D)),
        }
    _B_in = _B_setup()

    return {
        "A_x": _A_in["x"],
        "A_k_cache": _A_in["k_cache"],
        "A_v_cache": _A_in["v_cache"],
        "A_wq": _A_in["wq"],
        "A_wo": _A_in["wo"],
        "B_x": _B_in["x"],
        "B_w_gate": _B_in["w_gate"],
        "B_w_up": _B_in["w_up"],
        "B_w_down": _B_in["w_down"],
    }

def reference(A_x, A_k_cache, A_v_cache, A_wq, A_wo, B_x, B_w_gate, B_w_up, B_w_down):
    def _A_ref(x, k_cache, v_cache, wq, wo):
        B, Sq, D = x.shape
        H, Dh = 32, 128
        q = (lambda x, *d: x.permute(*d))((x @ wq).reshape(B, Sq, H, Dh), 0, 2, 1, 3)
        scores = torch.einsum("bhqd,bhkd->bhqk", q, k_cache) * (Dh ** -0.5)
        p = _softmax(scores, axis=-1)
        o = torch.einsum("bhqk,bhkd->bhqd", p, v_cache)
        return (lambda x, *d: x.permute(*d))(o, 0, 2, 1, 3).reshape(B, Sq, H * Dh) @ wo

    def _B_ref(x, w_gate, w_up, w_down):
        g = _silu(x @ w_gate)
        u = x @ w_up
        return (g * u) @ w_down

    x = A_x
    xn = _layernorm(x, axis=-1)
    s = _A_ref(xn, A_k_cache, A_v_cache, A_wq, A_wo)
    g = _silu(_B_ref(xn, B_w_gate, B_w_up, B_w_down))
    return x + s * g
```

```cpp
#include <hip/hip_runtime.h>
#include <stdint.h>

#define GAS __attribute__((address_space(1)))
#define LAS __attribute__((address_space(3)))
typedef unsigned u32x4 __attribute__((ext_vector_type(4)));
typedef float f32x4 __attribute__((ext_vector_type(4)));
typedef float f32x2 __attribute__((ext_vector_type(2)));
typedef __bf16 bf16x2 __attribute__((ext_vector_type(2)));
typedef unsigned short u16;

constexpr int WIN = 20;
constexpr int INITWIN = 60, INITG = 15;
constexpr int TAILBASE = 26, TAILMIN = 20, TAILMAX = 40;
constexpr float TAILGAIN = 18.f;
constexpr float GAIN = 40.f, DAMP = 0.7f;
constexpr int LASTPT = 440;
constexpr int ETA_J = 28;
constexpr int PRE = 4;
constexpr int QSLEEP = 15;

constexpr int DM = 512, NH = 32, DH = 128, FF = 1792;
constexpr int NWG = 256, NC = 8, NWAVE = NC + 3, NTHR = NWAVE * 64;
constexpr int NGRP = 256;
constexpr int NSLOT = 32;
constexpr int NFFN = 28;
constexpr float L2E = 1.4426950408889634f;

constexpr int RING_OFF = 0;
constexpr int SC_OFF = NSLOT * 4096;
constexpr int QV_OFF = SC_OFF + 16384;
constexpr int XT_OFF = QV_OFF + 256;
constexpr int YP_OFF = XT_OFF + 512;
constexpr int FX_OFF = YP_OFF + 4096;
constexpr int HT_OFF = FX_OFF + 4096;
constexpr int MM_OFF = HT_OFF + 512;
constexpr int PUB_OFF = MM_OFF + 64;
constexpr int NXT_OFF = PUB_OFF + 256;
constexpr int CNT_OFF = NXT_OFF + 32;
constexpr int LDS_BYTES = CNT_OFF + 64;
static_assert(LDS_BYTES <= 163840, "LDS");
enum { F_Q = 0, C_K = 2, C_L = 3, C_M = 4, C_E = 5, C_O = 6, W_FLAG = 7, W_SCALE = 8, W_MODE = 9, W_ELO = 10, W_EHI = 11 };

constexpr unsigned WS_EPOCH = 0;
constexpr unsigned WS_ETA = 256;
constexpr int NCTL = 6;
constexpr unsigned WS_PROG = WS_ETA + NWG * 8;
constexpr unsigned WS_ML = WS_PROG + NCTL * NWG * 8;
constexpr unsigned WS_Q = WS_ML + NWG * 16;
constexpr unsigned WS_G = WS_Q + 32 * 8 * 128 * 16;
constexpr unsigned WS_O = WS_G + 64 * 224 * 16;
constexpr unsigned WS_Y = WS_O + NWG * 64 * 16;
constexpr unsigned WS_END = WS_Y + 64 * 256 * 16;

constexpr unsigned long long SPIN_TICKS = 10000000ull;

struct Args {
    const u16* x; const u16* kc; const u16* vc; const u16* wq; const u16* wo; const u16* wg; const u16* wu; const u16* wd;
    u16* out; unsigned char* ws;
};

__device__ __forceinline__ float bf2f(unsigned v) { return __uint_as_float(v << 16); }
__device__ __forceinline__ float lo2f(unsigned v) { return __uint_as_float(v << 16); }
__device__ __forceinline__ float hi2f(unsigned v) { return __uint_as_float(v & 0xffff0000u); }
__device__ __forceinline__ unsigned f2bfbits(float f) { return (unsigned)__builtin_bit_cast(unsigned short, (__bf16)f); }
__device__ __forceinline__ float bfr(float f) { return __uint_as_float(f2bfbits(f) << 16); }
__device__ __forceinline__ float dot2(unsigned k, unsigned q, float acc) {
    return __builtin_amdgcn_fdot2_f32_bf16(__builtin_bit_cast(bf16x2, k), __builtin_bit_cast(bf16x2, q), acc, false);
}
template <int CTRL> __device__ __forceinline__ float dpp(float x) {
    return __builtin_bit_cast(float, __builtin_amdgcn_mov_dpp(__builtin_bit_cast(int, x), CTRL, 0xf, 0xf, true));
}
constexpr int X1 = 0xB1, X2 = 0x4E, X7 = 0x141, X15 = 0x140, X8 = 0x128;
__device__ __forceinline__ float tr16(float x, float y) { auto s = __builtin_amdgcn_permlane16_swap(__float_as_uint(x), __float_as_uint(y), false, false); return __uint_as_float(s[0]) + __uint_as_float(s[1]); }
__device__ __forceinline__ float tr32(float x, float y) { auto s = __builtin_amdgcn_permlane32_swap(__float_as_uint(x), __float_as_uint(y), false, false); return __uint_as_float(s[0]) + __uint_as_float(s[1]); }
__device__ __forceinline__ float sum16(float x) { return tr16(x, x); }
__device__ __forceinline__ float sum32(float x) { return tr32(x, x); }
__device__ __forceinline__ float max16(float x) { auto s = __builtin_amdgcn_permlane16_swap(__float_as_uint(x), __float_as_uint(x), false, false); return fmaxf(__uint_as_float(s[0]), __uint_as_float(s[1])); }
__device__ __forceinline__ float max32(float x) { auto s = __builtin_amdgcn_permlane32_swap(__float_as_uint(x), __float_as_uint(x), false, false); return fmaxf(__uint_as_float(s[0]), __uint_as_float(s[1])); }
__device__ __forceinline__ float wave_sum(float x) { x += dpp<X1>(x); x += dpp<X2>(x); x += dpp<X7>(x); x += dpp<X15>(x); return sum32(sum16(x)); }
__device__ __forceinline__ float wave_max(float x) { x = fmaxf(x, dpp<X1>(x)); x = fmaxf(x, dpp<X2>(x)); x = fmaxf(x, dpp<X7>(x)); x = fmaxf(x, dpp<X15>(x)); return max32(max16(x)); }
#define TR_STEP(x, HALF, CTRL, BIT, c) _Pragma("unroll") for (int h_ = 0; h_ < (HALF); ++h_) { \
        const float t1_ = x[h_] + dpp<CTRL>(x[h_]), t2_ = x[h_ + (HALF)] + dpp<CTRL>(x[h_ + (HALF)]); x[h_] = ((c) & (BIT)) ? t2_ : t1_; }

__device__ __forceinline__ float silu_f(float v) { return v / (1.f + __expf(-v)); }
__device__ __forceinline__ unsigned long long rt() { return __builtin_amdgcn_s_memrealtime(); }

__device__ __forceinline__ void lds_st(unsigned addr, unsigned v) { asm volatile("ds_write_b32 %0, %1" :: "v"(addr), "v"(v) : "memory"); }
__device__ __forceinline__ unsigned lds_ld(unsigned addr) { unsigned v; asm volatile("ds_read_b32 %0, %1\n\ts_waitcnt lgkmcnt(0)" : "=v"(v) : "v"(addr) : "memory"); return v; }
__device__ __forceinline__ void lds_add1(unsigned addr) { const unsigned one = 1u; asm volatile("ds_add_u32 %0, %1" :: "v"(addr), "v"(one) : "memory"); }
#define LGKM0() asm volatile("s_waitcnt lgkmcnt(0)" ::: "memory")
#define CBAR() asm volatile("" ::: "memory")

struct Key { unsigned a, b; };
__device__ __forceinline__ u32x4 mkrec(const Key& k, float v0, float v1) { const unsigned a = __float_as_uint(v0), b = __float_as_uint(v1); return u32x4{a, b, a ^ k.a, b ^ k.b}; }
__device__ __forceinline__ bool recok(const Key& k, const u32x4& r) { return (r.x ^ r.z) == k.a && (r.y ^ r.w) == k.b; }
__device__ __forceinline__ unsigned long long mix64(unsigned long long z) {
    z += 0x9E3779B97F4A7C15ull; z = (z ^ (z >> 30)) * 0xBF58476D1CE4E5B9ull; z = (z ^ (z >> 27)) * 0x94D049BB133111EBull; return z ^ (z >> 31);
}
__device__ __forceinline__ Key make_key(unsigned long long G, unsigned long long& E) {
    E = mix64(G);
    const unsigned elo = __builtin_amdgcn_readfirstlane((unsigned)E), ehi = __builtin_amdgcn_readfirstlane((unsigned)(E >> 32));
    E = ((unsigned long long)ehi << 32) | elo;
    return Key{elo | 1u, ehi | 1u};
}
__device__ __forceinline__ Key lds_key(unsigned cnt, unsigned long long& E) {
    const unsigned elo = __builtin_amdgcn_readfirstlane(lds_ld(cnt + 4 * W_ELO)), ehi = __builtin_amdgcn_readfirstlane(lds_ld(cnt + 4 * W_EHI));
    E = ((unsigned long long)ehi << 32) | elo;
    return Key{elo | 1u, ehi | 1u};
}
__device__ __forceinline__ u32x4 rec_ld(__amdgpu_buffer_rsrc_t rs, unsigned off) { return __builtin_amdgcn_raw_buffer_load_b128(rs, off, 0, 16); }
__device__ __forceinline__ void rec_st(__amdgpu_buffer_rsrc_t rs, unsigned off, u32x4 v) { __builtin_amdgcn_raw_buffer_store_b128(v, rs, off, 0, 16); }

struct Spin {
    unsigned long long t0; bool dead;
    __device__ __forceinline__ bool expired(unsigned& it) {
        if (dead) return true;
        if ((++it & 63u) == 0u && rt() - t0 > SPIN_TICKS) dead = true;
        return dead;
    }
};
template <int SLEEP = 1>
__device__ __forceinline__ unsigned lds_wait_ge(unsigned addr, unsigned want, Spin& sp) {
    unsigned it = 0, v;
    for (;;) {
        v = __builtin_amdgcn_readfirstlane(lds_ld(addr));
        if ((int)(v - want) >= 0) break;
        if (sp.expired(it)) break;
        if constexpr (SLEEP > 0) __builtin_amdgcn_s_sleep(SLEEP);
    }
    return v;
}
__device__ __forceinline__ void arrive(unsigned addr, int lane) { LGKM0(); if (lane == 0) lds_add1(addr); }

template <int D>
__device__ __forceinline__ void issue_group(unsigned m0v, unsigned voff, __amdgpu_buffer_rsrc_t rs, unsigned soff) {
    asm volatile("s_mov_b32 m0, %0\n\t"
                 "s_waitcnt vmcnt(%4)\n\tbuffer_load_dwordx4 %1, %2, %3 offen nt lds\n\t"
                 "s_waitcnt vmcnt(%4)\n\tbuffer_load_dwordx4 %1, %2, %3 offen offset:1024 nt lds\n\t"
                 "s_waitcnt vmcnt(%4)\n\tbuffer_load_dwordx4 %1, %2, %3 offen offset:2048 nt lds\n\t"
                 "s_waitcnt vmcnt(%4)\n\tbuffer_load_dwordx4 %1, %2, %3 offen offset:3072 nt lds"
                 :: "s"(m0v), "v"(voff), "s"(rs), "s"(soff), "i"(D - 1) : "memory");
}
template <int K> __device__ __forceinline__ void vm_wait() { asm volatile("s_waitcnt vmcnt(%0)" :: "i"(K) : "memory"); }

struct Loader {
    unsigned ring, voff, pubaddr, nxtaddr, modeaddr;
    int lim, pub;
};
__device__ __forceinline__ void ring_room(Loader& ld, int n, int need, Spin& sp) {
    bool drained = false; unsigned it = 0;
    for (;;) {
        u32x4 p, q;
        asm volatile("ds_read_b128 %0, %2\n\tds_read_b128 %1, %2 offset:16\n\ts_waitcnt lgkmcnt(0)" : "=&v"(p), "=&v"(q) : "v"(ld.nxtaddr) : "memory");
        const unsigned mn = min(min(min(p.x, p.y), min(p.z, p.w)), min(min(q.x, q.y), min(q.z, q.w)));
        ld.lim = (int)__builtin_amdgcn_readfirstlane(mn) + NSLOT;
        if (need < ld.lim) break;
        if (!drained) { vm_wait<0>(); ld.pub = n; lds_st(ld.pubaddr, (unsigned)n); drained = true; }
        if (sp.expired(it)) { ld.lim = 1 << 30; break; }
        __builtin_amdgcn_s_sleep(1);
    }
}
constexpr int BLK = 8;
template <int D>
__device__ __forceinline__ void load_block(Loader& ld, int n0, __amdgpu_buffer_rsrc_t rs) {
    unsigned m0v = ld.ring + (unsigned)(n0 & (NSLOT - 1)) * 4096u, soff = (unsigned)(n0 & (NGRP - 1)) * 4096u;
    int l = n0 + 1 - (D + 3) / 4;
#pragma unroll 2
    for (int i = 0; i < BLK; ++i) {
        issue_group<D>(m0v, ld.voff, rs, soff);
        if (l > ld.pub) { ld.pub = l; lds_st(ld.pubaddr, (unsigned)l); }
        m0v += 4096u; soff += 4096u; ++l;
    }
}
template <int LO, int HI>
__device__ __forceinline__ void load_block_d(Loader& ld, int n, __amdgpu_buffer_rsrc_t rs, int d) {
    if constexpr (LO == HI) load_block<LO>(ld, n, rs);
    else { constexpr int MID = (LO + HI) / 2; if (d <= MID) load_block_d<LO, MID>(ld, n, rs, d); else load_block_d<MID + 1, HI>(ld, n, rs, d); }
}
constexpr int DMIN = WIN < TAILMIN ? WIN : TAILMIN, DMAX = TAILMAX;
__device__ __forceinline__ void load_range(Loader& ld, int n0, int n1, __amdgpu_buffer_rsrc_t rs, Spin& sp) {
    for (int n = n0; n < n1; n += BLK) {
        if (n + BLK - 1 >= ld.lim) ring_room(ld, n, n + BLK - 1, sp);
        const int d = (int)__builtin_amdgcn_readfirstlane(lds_ld(ld.modeaddr));
        load_block_d<DMIN, DMAX>(ld, n, rs, d);
    }
}
template <int K> __device__ __forceinline__ void drain(Loader& ld) {
    if constexpr (K >= 0) {
        vm_wait<4 * K>();
        const int l = 2 * NGRP - K;
        if (l > ld.pub) { ld.pub = l; lds_st(ld.pubaddr, (unsigned)l); }
        drain<K - 1>(ld);
    }
}

__device__ __forceinline__ void ln_stats(const u32x4& xs, float& mb, float& den) {
    float v[8];
#pragma unroll
    for (int j = 0; j < 4; ++j) { v[2 * j] = lo2f(xs[j]); v[2 * j + 1] = hi2f(xs[j]); }
    float s = 0.f;
#pragma unroll
    for (int j = 0; j < 8; ++j) s += v[j];
    const float mean = wave_sum(s) * (1.f / DM);
    float q = 0.f;
#pragma unroll
    for (int j = 0; j < 8; ++j) { const float d = v[j] - mean; q = fmaf(d, d, q); }
    const float var = wave_sum(q) * (1.f / DM);
    mb = bfr(mean); den = bfr(sqrtf(bfr(bfr(var) + 1e-5f)));
}
__device__ __forceinline__ float xnorm(float xv, float mb, float den) { return bfr(bfr(xv - mb) / den); }
__device__ __forceinline__ u32x4 ld16(const u16* p) { return *(const GAS u32x4*)p; }
__device__ __forceinline__ unsigned ld2(const u16* p) { return *(const GAS u16*)p; }
__device__ __forceinline__ u32x4 ntload(const u16* p) { return __builtin_nontemporal_load((const GAS u32x4*)p); }

__global__ void __launch_bounds__(NTHR, 3) fused_decode_kernel(Args a) {
    __shared__ __attribute__((aligned(16))) unsigned char lds[LDS_BYTES];
    const int tid = threadIdx.x, lane = tid & 63, wave = __builtin_amdgcn_readfirstlane(tid >> 6);
    const int bid = blockIdx.x, xcd = bid & 7, idx = bid >> 3;
    const int sp_ = idx & 3, h = xcd + 8 * ((idx >> 2) & 3), b = idx >> 4, bh = b * NH + h, g = bh * 4 + sp_;
    LAS unsigned char* L = (LAS unsigned char*)lds;
    const unsigned lbase = (unsigned)(size_t)L;
    const unsigned cnt = lbase + CNT_OFF;
    Spin spin{rt(), false};

    if (__builtin_expect(wave == 0, 1)) {
        __builtin_amdgcn_s_setprio(3);
        __builtin_amdgcn_s_waitcnt(0);
        const size_t reg = (size_t)g * 4096 * DH;
        const __amdgpu_buffer_rsrc_t rsK = __builtin_amdgcn_make_buffer_rsrc((void*)(a.kc + reg), 0, 1 << 20, 0x00020000);
        const __amdgpu_buffer_rsrc_t rsV = __builtin_amdgcn_make_buffer_rsrc((void*)(a.vc + reg), 0, 1 << 20, 0x00020000);
        Loader ld{lbase + RING_OFF, (unsigned)lane * 16u, lbase + PUB_OFF + lane * 4, lbase + NXT_OFF, cnt + 4 * W_MODE, NSLOT, 0};
#pragma unroll 1
        for (int n = 0; n < INITG; ++n) issue_group<INITWIN>(ld.ring + n * 4096u, ld.voff, rsK, n * 4096u);
        __builtin_amdgcn_s_barrier();
        CBAR();
#pragma unroll 1
        for (int n = INITG; n < NSLOT; ++n) { issue_group<INITWIN>(ld.ring + n * 4096u, ld.voff, rsK, n * 4096u); const int l = n + 1 - (INITWIN + 3) / 4; if (l > ld.pub) { ld.pub = l; lds_st(ld.pubaddr, (unsigned)l); } }
#pragma unroll 1
        for (int ph = 0; ph < 2; ++ph) {
            load_range(ld, ph ? NGRP : NSLOT, ph ? 2 * NGRP : NGRP, ph ? rsV : rsK, spin);
        }
        drain<(DMAX + 3) / 4 - 1>(ld);
        return;
    }

    const __amdgpu_buffer_rsrc_t rsW = __builtin_amdgcn_make_buffer_rsrc((void*)a.ws, 0, WS_END, 0x00020000);
    const int c = lane & 15, r = lane >> 4;

    if (wave == NC + 2) {
        if (lane < 8) lds_st(lbase + NXT_OFF + lane * 4, (unsigned)lane);
        if (lane < 16) lds_st(cnt + lane * 4, lane == W_MODE ? (unsigned)WIN : 0u);
        lds_st(lbase + PUB_OFF + lane * 4, 0u);
        LGKM0();
        const int part = b * 4 + sp_;
        const u32x4 xs0 = ld16(a.x + 8 * lane), xs1 = ld16(a.x + DM + 8 * lane);
        const unsigned xr0 = ld2(a.x + 64 * part + lane), xr1 = ld2(a.x + DM + 64 * part + lane);
        u32x4 wqv[16];
#pragma unroll
        for (int i = 0; i < 16; ++i) wqv[i] = ntload(a.wq + (size_t)(64 * part + 4 * i + r) * (NH * DH) + h * DH + 8 * c);
        const unsigned long long G = __hip_atomic_load((GAS unsigned long long*)(a.ws + WS_EPOCH), __ATOMIC_RELAXED, __HIP_MEMORY_SCOPE_AGENT);
        __builtin_amdgcn_s_barrier();
        CBAR();
        unsigned long long E;
        Key key;
        {
            float mb0, den0, mb1, den1;
            ln_stats(xs0, mb0, den0); ln_stats(xs1, mb1, den1);
            *(LAS float*)(L + XT_OFF + ((lane & 3) * 16 + (lane >> 2)) * 4) = xnorm(bf2f(xr0), mb0, den0);
            *(LAS float*)(L + XT_OFF + (64 + (lane & 3) * 16 + (lane >> 2)) * 4) = xnorm(bf2f(xr1), mb1, den1);
            LGKM0();
            f32x2 qa[8];
#pragma unroll
            for (int j = 0; j < 8; ++j) qa[j] = f32x2{0.f, 0.f};
#pragma unroll
            for (int i4 = 0; i4 < 4; ++i4) {
                const f32x4 n0 = *(LAS f32x4*)(L + XT_OFF + (r * 16 + 4 * i4) * 4), n1 = *(LAS f32x4*)(L + XT_OFF + (64 + r * 16 + 4 * i4) * 4);
#pragma unroll
                for (int i = 0; i < 4; ++i)
#pragma unroll
                    for (int j = 0; j < 4; ++j) {
                        const f32x2 wv = f32x2{lo2f(wqv[4 * i4 + i][j]), hi2f(wqv[4 * i4 + i][j])};
                        qa[j] = __builtin_elementwise_fma(f32x2{n0[i], n0[i]}, wv, qa[j]); qa[4 + j] = __builtin_elementwise_fma(f32x2{n1[i], n1[i]}, wv, qa[4 + j]);
                    }
            }
            float z[4];
#pragma unroll
            for (int j = 0; j < 2; ++j) {
                const float a0 = tr16(qa[j][0], qa[4 + j][0]), a1 = tr16(qa[j][1], qa[4 + j][1]), b0 = tr16(qa[2 + j][0], qa[6 + j][0]), b1 = tr16(qa[2 + j][1], qa[6 + j][1]);
                z[2 * j] = tr32(a0, b0); z[2 * j + 1] = tr32(a1, b1);
            }
            key = make_key(G, E);
            const unsigned qoff = WS_Q + ((h * 8 + part) * 128 + (r & 1) * 64 + 4 * c + 2 * (r >> 1)) * 16;
            rec_st(rsW, qoff, mkrec(key, z[0], z[1])); rec_st(rsW, qoff + 16, mkrec(key, z[2], z[3]));
            __builtin_amdgcn_s_sleep(QSLEEP);
            u32x4 qr[8]; unsigned it = 0;
            for (;;) {
                CBAR();
                bool ok = true;
#pragma unroll
                for (int p = 0; p < 8; ++p) qr[p] = rec_ld(rsW, WS_Q + ((h * 8 + p) * 128 + b * 64 + lane) * 16);
#pragma unroll
                for (int p = 0; p < 8; ++p) ok = ok && recok(key, qr[p]);
                if (__all(ok)) break;
                if (spin.expired(it)) break;
                __builtin_amdgcn_s_sleep(8);
            }
            float q0 = 0.f, q1 = 0.f;
#pragma unroll
            for (int p = 0; p < 8; ++p) { q0 += __uint_as_float(qr[p].x); q1 += __uint_as_float(qr[p].y); }
            lds_st(lbase + QV_OFF + lane * 4, f2bfbits(q0) | (f2bfbits(q1) << 16));
            lds_st(cnt + 4 * (W_ELO + (lane & 1)), (lane & 1) ? (unsigned)(E >> 32) : (unsigned)E);
            LGKM0();
            lds_st(cnt + 4 * F_Q, 1u);
        }
        bool ml_out = false, scale_done = false;
        unsigned it = 0;
        int kc = 0, phase = 0;
        float lag_prev = 0.f; int d_prev = WIN; unsigned t_own = 0;
        int nap = 0;
        const int ppos = (idx & 7) * 32 + (idx >> 3) * 8 + xcd;
        for (;;) {
            const int landed = (int)__builtin_amdgcn_readfirstlane(lds_ld(lbase + PUB_OFF));
            if (!ml_out && (int)__builtin_amdgcn_readfirstlane(lds_ld(cnt + 4 * C_L)) >= NC) {
                const f32x4 l0 = *(LAS f32x4*)(L + MM_OFF + 32), l1 = *(LAS f32x4*)(L + MM_OFF + 48);
                const f32x4 m0 = *(LAS f32x4*)(L + MM_OFF), m1 = *(LAS f32x4*)(L + MM_OFF + 16);
                const float M = fmaxf(fmaxf(fmaxf(m0[0], m0[1]), fmaxf(m0[2], m0[3])), fmaxf(fmaxf(m1[0], m1[1]), fmaxf(m1[2], m1[3])));
                const float Ls = ((l0[0] + l0[1]) + (l0[2] + l0[3])) + ((l1[0] + l1[1]) + (l1[2] + l1[3]));
                if (lane == 0) rec_st(rsW, WS_ML + g * 16, mkrec(key, M, Ls));
                ml_out = true; nap = 12;
            }
            if (ml_out && !scale_done && --nap < 0) {
                CBAR();
                const u32x4 rm = rec_ld(rsW, WS_ML + ((g & ~3) + (lane & 3)) * 16);
                if (__all(recok(key, rm))) {
                    const float ms = __uint_as_float(rm.x), ls = __uint_as_float(rm.y);
                    float M = fmaxf(ms, dpp<X1>(ms)); M = fmaxf(M, dpp<X2>(M));
                    const float e = __builtin_amdgcn_exp2f((ms - M) * L2E);
                    float Ls = e * ls; Ls += dpp<X1>(Ls); Ls += dpp<X2>(Ls);
                    if (lane == sp_) { lds_st(cnt + 4 * W_SCALE, __float_as_uint(e / Ls)); LGKM0(); lds_st(cnt + 4 * W_FLAG, 1u); }
                    scale_done = true;
                } else nap = 8;
            }
            if (scale_done && kc >= NCTL) break;
            if (spin.expired(it)) { if (lane == 0) lds_st(cnt + 4 * W_FLAG, 1u); break; }
            if (kc < NCTL) {
                const int pt = kc < 5 ? 64 + 84 * kc : LASTPT;
                if (phase == 0 && landed >= pt) {
                    t_own = (unsigned)rt();
                    if (lane == 0) __hip_atomic_store((GAS unsigned long long*)(a.ws + WS_PROG) + kc * NWG + ppos, ((unsigned long long)key.a << 32) | t_own, __ATOMIC_RELAXED, __HIP_MEMORY_SCOPE_AGENT);
                    phase = 1;
                } else if (phase == 1 && landed >= pt + (kc < 5 ? 20 : 12)) {
                    CBAR();
                    const unsigned long long pv = __hip_atomic_load((GAS unsigned long long*)(a.ws + WS_PROG) + kc * NWG + ((ppos & ~31) + (lane & 31)), __ATOMIC_RELAXED, __HIP_MEMORY_SCOPE_AGENT);
                    const bool ok = (unsigned)(pv >> 32) == key.a && lane < 32;
                    const float dd = ok ? (float)(int)(t_own - (unsigned)pv) : 0.f;
                    const float nv = wave_sum(ok ? 1.f : 0.f), sd = wave_sum(dd);
                    int d = d_prev;
                    if (nv >= 12.f) {
                        const float lag = sd / nv;
                        if (kc < 5) {
                            const float drift = kc == 0 ? 0.f : lag - lag_prev + GAIN * (float)(d_prev - WIN);
                            d = WIN + (int)rintf(DAMP * (lag + drift) / GAIN);
                            d = d < WIN ? WIN : d > WIN + 8 ? WIN + 8 : d;
                        } else {
                            d = TAILBASE + (int)rintf(lag / TAILGAIN);
                            d = d < TAILMIN ? TAILMIN : d > DMAX ? DMAX : d;
                        }
                        lag_prev = lag;
                    } else if (kc == 5) d = nv >= 1.f ? TAILMIN : DMAX;
                    lds_st(cnt + 4 * W_MODE, (unsigned)d);
                    d_prev = d;
                    ++kc; phase = 0;
                }
            }
            __builtin_amdgcn_s_sleep(6);
        }
        lds_wait_ge<2>(lbase + PUB_OFF, 2 * NGRP - 16, spin);
        it = 0;
        for (bool first = true;; first = false) {
            CBAR();
            const unsigned long long e = __hip_atomic_load((GAS unsigned long long*)(a.ws + WS_ETA) + (g & ~3) + (lane & 3), __ATOMIC_RELAXED, __HIP_MEMORY_SCOPE_AGENT);
            if (__all(e == E)) break;
            if (spin.expired(it)) break;
            if (first) lds_wait_ge<2>(lbase + PUB_OFF, 2 * NGRP, spin);
            __builtin_amdgcn_s_sleep(1);
        }
        lds_st(cnt + 4 * C_E, 1u);
        __builtin_amdgcn_endpgm();
    }

    if (wave == NC + 1) {
        const bool is_ffn = bid >= NWG - NFFN, is_fin = bid < 64;
        __builtin_amdgcn_s_barrier();
        CBAR();
        if (!is_ffn && !is_fin) __builtin_amdgcn_endpgm();
        unsigned long long E;
        if (is_ffn) {
            const int cg = bid - (NWG - NFFN);
            const u32x4 xs0 = ld16(a.x + 8 * lane), xs1 = ld16(a.x + DM + 8 * lane);
            {
                float mb0, den0, mb1, den1;
                ln_stats(xs0, mb0, den0); ln_stats(xs1, mb1, den1);
#pragma unroll
                for (int j = 0; j < 8; ++j) {
                    *(LAS float*)(L + FX_OFF + (j * 64 + lane) * 4) = xnorm((j & 1) ? hi2f(xs0[j >> 1]) : lo2f(xs0[j >> 1]), mb0, den0);
                    *(LAS float*)(L + FX_OFF + ((8 + j) * 64 + lane) * 4) = xnorm((j & 1) ? hi2f(xs1[j >> 1]) : lo2f(xs1[j >> 1]), mb1, den1);
                }
                LGKM0();
            }
            lds_wait_ge<4>(cnt + 4 * F_Q, 1, spin);
            const Key key = lds_key(cnt, E);
            const int r8 = lane >> 3, cc = lane & 7;
            float fa[32];
#pragma unroll
            for (int j = 0; j < 32; ++j) fa[j] = 0.f;
            const u16* pg = a.wg + (size_t)r8 * FF + cg * 64 + cc * 8;
            const u16* pu = a.wu + (size_t)r8 * FF + cg * 64 + cc * 8;
            u32x4 wg[4], wu[4];
#pragma unroll
            for (int i = 0; i < 4; ++i) { wg[i] = ntload(pg + (size_t)i * 8 * FF); wu[i] = ntload(pu + (size_t)i * 8 * FF); }
#pragma unroll 1
            for (int i0 = 0; i0 < 64; i0 += 4) {
                const f32x4 n0 = *(LAS f32x4*)(L + FX_OFF + (r8 * 64 + i0) * 4), n1 = *(LAS f32x4*)(L + FX_OFF + ((8 + r8) * 64 + i0) * 4);
                const int inx = i0 + 4 < 64 ? i0 + 4 : i0;
#pragma unroll
                for (int i = 0; i < 4; ++i) {
                    const u32x4 g4 = wg[i], u4 = wu[i];
                    wg[i] = ntload(pg + (size_t)(inx + i) * 8 * FF); wu[i] = ntload(pu + (size_t)(inx + i) * 8 * FF);
#pragma unroll
                    for (int j = 0; j < 4; ++j) {
                        const float g0 = lo2f(g4[j]), g1 = hi2f(g4[j]), u0 = lo2f(u4[j]), u1 = hi2f(u4[j]);
                        fa[2 * j] = fmaf(n0[i], g0, fa[2 * j]); fa[2 * j + 1] = fmaf(n0[i], g1, fa[2 * j + 1]);
                        fa[8 + 2 * j] = fmaf(n1[i], g0, fa[8 + 2 * j]); fa[8 + 2 * j + 1] = fmaf(n1[i], g1, fa[8 + 2 * j + 1]);
                        fa[16 + 2 * j] = fmaf(n0[i], u0, fa[16 + 2 * j]); fa[16 + 2 * j + 1] = fmaf(n0[i], u1, fa[16 + 2 * j + 1]);
                        fa[24 + 2 * j] = fmaf(n1[i], u0, fa[24 + 2 * j]); fa[24 + 2 * j + 1] = fmaf(n1[i], u1, fa[24 + 2 * j + 1]);
                    }
                }
            }
            TR_STEP(fa, 16, X8, 8, lane)
#pragma unroll
            for (int j = 0; j < 16; ++j) {
                const float v = sum32(sum16(fa[j])), u = dpp<X8>(v);
                const float hv = bfr(bfr(silu_f(bfr(v))) * bfr(u));
                if (lane < 8) *(LAS float*)(L + HT_OFF + ((j >> 3) * 64 + cc * 8 + (j & 7)) * 4) = hv;
            }
            LGKM0();
            f32x2 ga[8];
#pragma unroll
            for (int j = 0; j < 8; ++j) ga[j] = f32x2{0.f, 0.f};
            const u16* pd = a.wd + (size_t)cg * 64 * DM + 8 * lane;
            u32x4 wd[8];
#pragma unroll
            for (int i = 0; i < 8; ++i) wd[i] = ntload(pd + (size_t)i * DM);
#pragma unroll 1
            for (int i0 = 0; i0 < 64; i0 += 8) {
                const f32x4 ha = *(LAS f32x4*)(L + HT_OFF + i0 * 4), hb = *(LAS f32x4*)(L + HT_OFF + (i0 + 4) * 4);
                const f32x4 hc = *(LAS f32x4*)(L + HT_OFF + (64 + i0) * 4), hd = *(LAS f32x4*)(L + HT_OFF + (64 + i0 + 4) * 4);
                const int inx = i0 + 8 < 64 ? i0 + 8 : i0;
#pragma unroll
                for (int i = 0; i < 8; ++i) {
                    const u32x4 w4 = wd[i];
                    wd[i] = ntload(pd + (size_t)(inx + i) * DM);
                    const float h0 = i < 4 ? ha[i & 3] : hb[i & 3], h1 = i < 4 ? hc[i & 3] : hd[i & 3];
#pragma unroll
                    for (int j = 0; j < 4; ++j) {
                        const f32x2 wv = f32x2{lo2f(w4[j]), hi2f(w4[j])};
                        ga[j] = __builtin_elementwise_fma(f32x2{h0, h0}, wv, ga[j]); ga[4 + j] = __builtin_elementwise_fma(f32x2{h1, h1}, wv, ga[4 + j]);
                    }
                }
            }
#pragma unroll
            for (int q = 0; q < 4; ++q)
                *(LAS f32x4*)(L + FX_OFF + (lane * 16 + q * 4) * 4) = f32x4{ga[2 * q][0], ga[2 * q][1], ga[2 * q + 1][0], ga[2 * q + 1][1]};
            LGKM0();
#pragma unroll
            for (int t = 0; t < 8; ++t) {
                const int fo = 8 * t + (lane >> 3);
                const f32x2 v = *(LAS f32x2*)(L + FX_OFF + (fo * 16 + 2 * (lane & 7)) * 4);
                rec_st(rsW, WS_G + ((fo * NFFN + cg) * 8 + (lane & 7)) * 16, mkrec(key, v[0], v[1]));
            }
            __builtin_amdgcn_endpgm();
        }
        const int f = bid;
        lds_wait_ge<8>(cnt + 4 * F_Q, 1, spin);
        const Key key = lds_key(cnt, E);
        const int ob = (lane >> 2) & 1, op = lane & 3;
        const unsigned xres = *(const GAS unsigned*)(a.x + ob * DM + 8 * f + 2 * op);
        float gate0, gate1;
        lds_wait_ge<32>(lbase + PUB_OFF, 2 * NGRP - 64, spin);
        {
            u32x4 gr[4]; unsigned it = 0;
            for (;;) {
                CBAR();
                bool ok = true;
#pragma unroll
                for (int i = 0; i < 4; ++i) { const int ri = i * 64 + lane; gr[i] = rec_ld(rsW, WS_G + (f * 224 + (ri < 224 ? ri : lane)) * 16); }
#pragma unroll
                for (int i = 0; i < 4; ++i) ok = ok && recok(key, gr[i]);
                if (__all(ok)) break;
                if (spin.expired(it)) break;
                __builtin_amdgcn_s_sleep(32);
            }
            float s0 = 0.f, s1 = 0.f;
#pragma unroll
            for (int i = 0; i < 4; ++i) { const bool on = i * 64 + lane < 224; s0 += on ? __uint_as_float(gr[i].x) : 0.f; s1 += on ? __uint_as_float(gr[i].y) : 0.f; }
            s0 += dpp<X8>(s0); s1 += dpp<X8>(s1);
            gate0 = bfr(silu_f(bfr(sum32(sum16(s0))))); gate1 = bfr(silu_f(bfr(sum32(sum16(s1)))));
        }
        lds_wait_ge<2>(cnt + 4 * C_M, 1, spin);
        {
            unsigned it = 0;
            const unsigned elo = (unsigned)E, ehi = (unsigned)(E >> 32);
            for (;;) {
                CBAR();
                const u32x4 e0 = rec_ld(rsW, WS_ETA + lane * 32), e1 = rec_ld(rsW, WS_ETA + lane * 32 + 16);
                const bool ok = e0.x == elo && e0.z == elo && e1.x == elo && e1.z == elo && e0.y == ehi && e0.w == ehi && e1.y == ehi && e1.w == ehi;
                if (__all(ok)) break;
                if (spin.expired(it)) break;
                __builtin_amdgcn_s_sleep(4);
            }
        }
        u32x4 yr[4];
        {
            unsigned it = 0;
            u32x4 ya[4], yb[4];
            CBAR();
#pragma unroll
            for (int i = 0; i < 4; ++i) ya[i] = rec_ld(rsW, WS_Y + (f * 256 + i * 64 + lane) * 16);
            for (;;) {
                CBAR();
#pragma unroll
                for (int i = 0; i < 4; ++i) yb[i] = rec_ld(rsW, WS_Y + (f * 256 + i * 64 + lane) * 16);
                if (__all(recok(key, ya[0]) && recok(key, ya[1]) && recok(key, ya[2]) && recok(key, ya[3]))) {
#pragma unroll
                    for (int i = 0; i < 4; ++i) yr[i] = ya[i];
                    break;
                }
                CBAR();
#pragma unroll
                for (int i = 0; i < 4; ++i) ya[i] = rec_ld(rsW, WS_Y + (f * 256 + i * 64 + lane) * 16);
                if (__all(recok(key, yb[0]) && recok(key, yb[1]) && recok(key, yb[2]) && recok(key, yb[3])) || spin.expired(it)) {
#pragma unroll
                    for (int i = 0; i < 4; ++i) yr[i] = yb[i];
                    break;
                }
            }
        }
        float s0 = 0.f, s1 = 0.f;
#pragma unroll
        for (int i = 0; i < 4; ++i) { s0 += __uint_as_float(yr[i].x); s1 += __uint_as_float(yr[i].y); }
        s0 += dpp<X8>(s0); s1 += dpp<X8>(s1);
        s0 = bfr(sum32(sum16(s0))); s1 = bfr(sum32(sum16(s1)));
        const float o0 = lo2f(xres) + bfr(s0 * gate0), o1 = hi2f(xres) + bfr(s1 * gate1);
        const unsigned ov = spin.dead ? 0x7fc07fc0u : (f2bfbits(o0) | (f2bfbits(o1) << 16));
        if (lane < 8) *(GAS unsigned*)(a.out + ob * DM + 8 * f + 2 * op) = ov;
        if (f == 0 && lane == 0) __hip_atomic_store((GAS unsigned long long*)(a.ws + WS_EPOCH), E, __ATOMIC_RELAXED, __HIP_MEMORY_SCOPE_AGENT);
        __builtin_amdgcn_endpgm();
    }

    const int w = wave - 1;
    __builtin_amdgcn_s_barrier();
    CBAR();
    const unsigned pubw = lbase + PUB_OFF, mynxt = lbase + NXT_OFF + w * 4;
    LAS unsigned char* scw = L + SC_OFF + w * 2048;
    unsigned landed = 0;
    float mx = -INFINITY;
    u32x4 kp[PRE][4];
#pragma unroll
    for (int j = 0; j < PRE; ++j) {
        const int n = j * NC + w;
        if ((int)landed <= n) landed = lds_wait_ge(pubw, n + 1, spin);
        const LAS u32x4* slot = (const LAS u32x4*)(L + RING_OFF + (n & (NSLOT - 1)) * 4096) + lane;
#pragma unroll
        for (int i = 0; i < 4; ++i) kp[j][i] = slot[i * 64];
        LGKM0();
        lds_st(mynxt, n + NC);
#pragma unroll
        for (int i = 0; i < 4; ++i) asm volatile("" : "+v"(kp[j][i]));
    }
    lds_wait_ge(cnt + 4 * F_Q, 1, spin);
    unsigned long long E;
    const Key key = lds_key(cnt, E);
    const u32x4 qv = *(LAS u32x4*)(L + QV_OFF + c * 16);
    auto scores = [&](const u32x4 (&kk)[4], int j) {
        float d[4];
#pragma unroll
        for (int i = 0; i < 4; ++i) { float s = 0.f;
#pragma unroll
            for (int q = 0; q < 4; ++q) s = dot2(kk[i][q], qv[q], s);
            d[i] = s; }
        TR_STEP(d, 2, X8, 8, c) TR_STEP(d, 1, X7, 4, c)
        float s = d[0]; s += dpp<X2>(s); s += dpp<X1>(s);
        s = bfr(bfr(s) * 0.08838834764831845f);
        *(LAS float*)(scw + j * 64 + r * 16 + (c >> 2) * 4) = s;
        mx = fmaxf(mx, s);
    };
#pragma unroll
    for (int j = 0; j < PRE; ++j) scores(kp[j], j);
    for (int j = PRE; j < NGRP / NC; ++j) {
        const int n = j * NC + w;
        if ((int)landed <= n) landed = lds_wait_ge(pubw, n + 1, spin);
        const LAS u32x4* slot = (const LAS u32x4*)(L + RING_OFF + (n & (NSLOT - 1)) * 4096) + lane;
        u32x4 kk[4];
#pragma unroll
        for (int i = 0; i < 4; ++i) kk[i] = slot[i * 64];
        LGKM0();
        lds_st(mynxt, n + NC);
#pragma unroll
        for (int i = 0; i < 4; ++i) asm volatile("" : "+v"(kk[i]));
        scores(kk, j);
    }
    {
        mx = wave_max(mx);
        if (lane == 0) *(LAS float*)(L + MM_OFF + w * 4) = mx;
        arrive(cnt + 4 * C_K, lane);
        lds_wait_ge(cnt + 4 * C_K, NC, spin);
        const f32x4 m0 = *(LAS f32x4*)(L + MM_OFF), m1 = *(LAS f32x4*)(L + MM_OFF + 16);
        const float M = fmaxf(fmaxf(fmaxf(m0[0], m0[1]), fmaxf(m0[2], m0[3])), fmaxf(fmaxf(m1[0], m1[1]), fmaxf(m1[2], m1[3])));
        f32x4 s0 = *(LAS f32x4*)(scw + lane * 32), s1 = *(LAS f32x4*)(scw + lane * 32 + 16);
        float lsum = 0.f;
#pragma unroll
        for (int j = 0; j < 4; ++j) { s0[j] = __builtin_amdgcn_exp2f((s0[j] - M) * L2E); s1[j] = __builtin_amdgcn_exp2f((s1[j] - M) * L2E); lsum += s0[j] + s1[j]; }
        *(LAS f32x4*)(scw + lane * 32) = s0; *(LAS f32x4*)(scw + lane * 32 + 16) = s1;
        lsum = wave_sum(lsum);
        if (lane == 0) *(LAS float*)(L + MM_OFF + 32 + w * 4) = lsum;
        arrive(cnt + 4 * C_L, lane);
        LGKM0();
    }
    f32x2 acc[4];
#pragma unroll
    for (int j = 0; j < 4; ++j) acc[j] = f32x2{0.f, 0.f};
    float scale = 0.f;
    for (int j = 0; j < NGRP / NC; ++j) {
        const int n = NGRP + j * NC + w;
        if (j == NGRP / NC - 2) {
            lds_wait_ge<2>(cnt + 4 * W_FLAG, 1, spin);
            scale = __uint_as_float(lds_ld(cnt + 4 * W_SCALE));
        }
        if ((int)landed <= n) landed = j >= NGRP / NC - 2 ? lds_wait_ge<0>(pubw, n + 1, spin) : lds_wait_ge<1>(pubw, n + 1, spin);
        if (j == ETA_J && w == 0 && lane == 0) __hip_atomic_store((GAS unsigned long long*)(a.ws + WS_ETA) + g, E, __ATOMIC_RELAXED, __HIP_MEMORY_SCOPE_AGENT);
        const LAS u32x4* slot = (const LAS u32x4*)(L + RING_OFF + (n & (NSLOT - 1)) * 4096) + lane;
        u32x4 vv[4];
#pragma unroll
        for (int i = 0; i < 4; ++i) vv[i] = slot[i * 64];
        f32x4 p = *(LAS f32x4*)(scw + j * 64 + r * 16);
        LGKM0();
        lds_st(mynxt, n + NC);
#pragma unroll
        for (int i = 0; i < 4; ++i) asm volatile("" : "+v"(vv[i]));
#pragma unroll
        for (int i = 0; i < 4; ++i) { const f32x2 pp = f32x2{p[i], p[i]};
#pragma unroll
            for (int q = 0; q < 4; ++q) acc[q] = __builtin_elementwise_fma(pp, f32x2{lo2f(vv[i][q]), hi2f(vv[i][q])}, acc[q]); }
    }
    u32x4 wov[4];
#pragma unroll
    for (int i = 0; i < 4; ++i) wov[i] = ntload(a.wo + (size_t)(h * DH + 16 * w + 4 * r + i) * DM + sp_ * 128 + 8 * c);
    *(LAS f32x4*)(scw + (r * 128 + 8 * c) * 4) = f32x4{acc[0][0], acc[0][1], acc[1][0], acc[1][1]};
    *(LAS f32x4*)(scw + (r * 128 + 8 * c + 4) * 4) = f32x4{acc[2][0], acc[2][1], acc[3][0], acc[3][1]};
    arrive(cnt + 4 * C_M, lane);
    lds_wait_ge<0>(cnt + 4 * C_M, NC, spin);
    {
        float o = 0.f;
#pragma unroll
        for (int i = 0; i < 8; ++i) o += *(LAS float*)(L + SC_OFF + (2 * r + (i >> 2)) * 2048 + ((i & 3) * 128 + 16 * w + c) * 4);
        o = sum32(sum16(o)) * scale;
        const float on = dpp<X1>(o);
        if (lane < 16 && (lane & 1) == 0) rec_st(rsW, WS_O + (g * 64 + 8 * w + (c >> 1)) * 16, mkrec(key, o, on));
    }
    lds_wait_ge<0>(cnt + 4 * C_E, 1, spin);
    {
        u32x4 ro; unsigned it = 0;
        const unsigned off = WS_O + (((g & ~3) + ((lane >> 3) & 3)) * 64 + 8 * w + (lane & 7)) * 16;
        CBAR();
        u32x4 ra = rec_ld(rsW, off), rb;
        for (;;) {
            CBAR();
            rb = rec_ld(rsW, off);
            if (__all(recok(key, ra))) { ro = ra; break; }
            CBAR();
            ra = rec_ld(rsW, off);
            if (__all(recok(key, rb))) { ro = rb; break; }
            if (spin.expired(it)) { ro = rb; break; }
        }
        float o0 = __uint_as_float(ro.x), o1 = __uint_as_float(ro.y);
        o0 += dpp<X8>(o0); o1 += dpp<X8>(o1);
        o0 = bfr(sum16(o0)); o1 = bfr(sum16(o1));
        float ov[4];
        ov[0] = __int_as_float(__builtin_amdgcn_ds_bpermute((2 * r) * 4, __float_as_int(o0)));
        ov[1] = __int_as_float(__builtin_amdgcn_ds_bpermute((2 * r) * 4, __float_as_int(o1)));
        ov[2] = __int_as_float(__builtin_amdgcn_ds_bpermute((2 * r + 1) * 4, __float_as_int(o0)));
        ov[3] = __int_as_float(__builtin_amdgcn_ds_bpermute((2 * r + 1) * 4, __float_as_int(o1)));
        float ya[8];
#pragma unroll
        for (int j = 0; j < 8; ++j) ya[j] = 0.f;
#pragma unroll
        for (int i = 0; i < 4; ++i)
#pragma unroll
            for (int j = 0; j < 4; ++j) { ya[2 * j] = fmaf(ov[i], lo2f(wov[i][j]), ya[2 * j]); ya[2 * j + 1] = fmaf(ov[i], hi2f(wov[i][j]), ya[2 * j + 1]); }
#pragma unroll
        for (int j = 0; j < 4; ++j) ya[j] = tr16(ya[j], ya[4 + j]);
#pragma unroll
        for (int j = 0; j < 2; ++j) ya[j] = tr32(ya[j], ya[2 + j]);
        *(LAS f32x2*)(L + YP_OFF + (w * 128 + 8 * c + 4 * (r & 1) + 2 * (r >> 1)) * 4) = f32x2{ya[0], ya[1]};
    }
    arrive(cnt + 4 * C_O, lane);
    lds_wait_ge<0>(cnt + 4 * C_O, NC, spin);
    {
        float y = *(LAS float*)(L + YP_OFF + ((2 * r) * 128 + 16 * w + c) * 4) + *(LAS float*)(L + YP_OFF + ((2 * r + 1) * 128 + 16 * w + c) * 4);
        y = sum32(sum16(y));
        const float yn = dpp<X1>(y);
        const int fo = sp_ * 16 + 2 * w + (c >> 3);
        if (lane < 16 && (lane & 1) == 0) rec_st(rsW, WS_Y + (((fo * 32 + h) * 2 + b) * 4 + ((c & 7) >> 1)) * 16, mkrec(key, y, yn));
    }
}

extern "C" void kernel_launch(void* const* d_in, const int* in_sizes, int n_in, void* d_out, int out_size, void* d_ws, size_t ws_size, hipStream_t stream) {
    Args a;
    a.x = (const u16*)d_in[0]; a.kc = (const u16*)d_in[1]; a.vc = (const u16*)d_in[2]; a.wq = (const u16*)d_in[3]; a.wo = (const u16*)d_in[4];
    a.wg = (const u16*)d_in[6]; a.wu = (const u16*)d_in[7]; a.wd = (const u16*)d_in[8];
    a.out = (u16*)d_out;
    a.ws = (unsigned char*)d_ws + ((ws_size - (8u << 20)) & ~(size_t)0xFFFFF);
    hipLaunchKernelGGL(fused_decode_kernel, dim3(NWG), dim3(NTHR), 0, stream, a);
}
```
